```python
import torch

def _softmax(x, axis=-1):
    return torch.softmax(x, dim=axis)
def _gelu(x):
    return torch.nn.functional.gelu(x, approximate="tanh")
def _silu(x):
    return torch.nn.functional.silu(x)
def _sigmoid(x):
    return torch.sigmoid(x)
def _layernorm(x, axis=-1, eps=1e-5):
    m = x.mean(dim=axis, keepdim=True)
    v = x.var(dim=axis, keepdim=True, unbiased=False)
    return (x - m) / torch.sqrt(v + eps)
def _scatter_add(tgt, idx, src, axis=0):
    return tgt.clone().index_add_(axis, idx, src)
def _segment_sum(src, seg, n):
    out = torch.zeros((n,) + src.shape[1:], dtype=src.dtype)
    return out.index_add_(0, seg, src)
def _pad(x, pw):
    flat = tuple(v for pair in reversed(pw) for v in pair)
    return torch.nn.functional.pad(x, flat)

kernel_name = 'standalone__fused_bias_dropout_add__prompt'

def setup_inputs():

    _w = (lambda a: (lambda t: t.to(torch.bfloat16) if t.is_floating_point() else t)(torch.as_tensor(a)))
    B, S, D = 2, 2048, 1024
    keep = 0.9
    mask = (torch.rand(B, S, D) < keep).to(float)
    return {
        "x": _w((lambda *s: torch.randn(*s, dtype=torch.bfloat16) * 0.05)(B, S, D)),
        "bias": _w((lambda *s: torch.randn(*s, dtype=torch.bfloat16) * 0.05)(D)),
        "mask": _w(mask),
        "residual": _w((lambda *s: torch.randn(*s, dtype=torch.bfloat16) * 0.05)(B, S, D)),
    }

def reference(x, bias, mask, residual):
    keep = 0.9
    return residual + (x + bias) * mask * (1.0 / keep)
```

```cpp
#include <hip/hip_runtime.h>
#include <hip/hip_bf16.h>
#include <stdint.h>

typedef unsigned int u32x4 __attribute__((ext_vector_type(4)));
typedef float f32x2 __attribute__((ext_vector_type(2)));
typedef __bf16 bf16x2 __attribute__((ext_vector_type(2)));

#define BLOCK 256
#define UNROLL 1

__device__ __forceinline__ f32x2 unpack2(unsigned int v) {
    f32x2 r;
    r.x = __uint_as_float(v << 16);
    r.y = __uint_as_float(v & 0xffff0000u);
    return r;
}
__device__ __forceinline__ unsigned int pack2(f32x2 v) {
    bf16x2 b = __builtin_convertvector(v, bf16x2);
    return __builtin_bit_cast(unsigned int, b);
}

__device__ __forceinline__ unsigned int op2(unsigned int x, f32x2 b, unsigned int m, unsigned int r,
                                            float scale) {
    f32x2 t = unpack2(pack2(unpack2(x) + b));
    t = unpack2(pack2(t * unpack2(m)));
    t = unpack2(pack2(t * scale));
    return pack2(unpack2(r) + t);
}

__global__ __launch_bounds__(BLOCK) void fbda_vec_kernel(const u32x4* __restrict__ x,
                                                         const u32x4* __restrict__ bias,
                                                         const u32x4* __restrict__ mask,
                                                         const u32x4* __restrict__ res,
                                                         u32x4* __restrict__ out, unsigned nvec,
                                                         unsigned dvec_mask, float scale) {
    const unsigned base = blockIdx.x * (BLOCK * UNROLL) + threadIdx.x;
    u32x4 vx[UNROLL], vm[UNROLL], vr[UNROLL];
#pragma unroll
    for (int u = 0; u < UNROLL; ++u) {
        unsigned i = base + u * BLOCK;
        vx[u] = x[i];
        vm[u] = mask[i];
        vr[u] = res[i];
    }
    u32x4 vb = bias[base & dvec_mask];
    f32x2 b[4];
#pragma unroll
    for (int j = 0; j < 4; ++j) b[j] = unpack2(vb[j]);
#pragma unroll
    for (int u = 0; u < UNROLL; ++u) {
        unsigned i = base + u * BLOCK;
        u32x4 o;
#pragma unroll
        for (int j = 0; j < 4; ++j) o[j] = op2(vx[u][j], b[j], vm[u][j], vr[u][j], scale);
        out[i] = o;
    }
}

__global__ void fbda_generic_kernel(const unsigned short* __restrict__ x,
                                    const unsigned short* __restrict__ bias,
                                    const unsigned short* __restrict__ mask,
                                    const unsigned short* __restrict__ res,
                                    unsigned short* __restrict__ out, long start, long n, int D,
                                    float scale) {
    long i = start + (long)blockIdx.x * blockDim.x + threadIdx.x;
    long stride = (long)gridDim.x * blockDim.x;
    for (; i < n; i += stride) {
        f32x2 b;
        b.x = __uint_as_float((unsigned)bias[i % D] << 16);
        b.y = 0.f;
        unsigned o = op2(x[i], b, mask[i], res[i], scale);
        out[i] = (unsigned short)(o & 0xffffu);
    }
}

extern "C" void kernel_launch(void* const* d_in, const int* in_sizes, int n_in, void* d_out,
                              int out_size, void* d_ws, size_t ws_size, hipStream_t stream) {
    const long n = in_sizes[0];
    const int D = in_sizes[1];
    const float scale = (float)(1.0 / 0.9);
    const long per_block = (long)BLOCK * UNROLL * 8;
    const bool pow2 = D >= 8 && (D & (D - 1)) == 0 && (BLOCK * 8) % D == 0;
    const bool aligned = ((((uintptr_t)d_in[0]) | ((uintptr_t)d_in[1]) | ((uintptr_t)d_in[2]) |
                           ((uintptr_t)d_in[3]) | ((uintptr_t)d_out)) & 15) == 0;
    long done = 0;
    if (pow2 && aligned) {
        long nblk = n / per_block;
        if (nblk > 0) {
            fbda_vec_kernel<<<dim3((unsigned)nblk), dim3(BLOCK), 0, stream>>>(
                (const u32x4*)d_in[0], (const u32x4*)d_in[1], (const u32x4*)d_in[2],
                (const u32x4*)d_in[3], (u32x4*)d_out, (unsigned)(n / 8), (unsigned)(D / 8 - 1),
                scale);
            done = nblk * per_block;
        }
    }
    if (done < n) {
        long rem = n - done;
        long blocks = (rem + 255) / 256;
        if (blocks > 4096) blocks = 4096;
        fbda_generic_kernel<<<dim3((unsigned)blocks), dim3(256), 0, stream>>>(
            (const unsigned short*)d_in[0], (const unsigned short*)d_in[1],
            (const unsigned short*)d_in[2], (const unsigned short*)d_in[3],
            (unsigned short*)d_out, done, n, D, scale);
    }
}
```

```cpp
#include <hip/hip_runtime.h>
#include <hip/hip_bf16.h>
#include <stdint.h>

typedef unsigned int u32x4 __attribute__((ext_vector_type(4)));
typedef float f32x2 __attribute__((ext_vector_type(2)));
typedef __bf16 bf16x2 __attribute__((ext_vector_type(2)));

#define BLOCK 512
#define UNROLL 1

__device__ __forceinline__ f32x2 unpack2(unsigned int v) {
    f32x2 r;
    r.x = __uint_as_float(v << 16);
    r.y = __uint_as_float(v & 0xffff0000u);
    return r;
}
__device__ __forceinline__ unsigned int pack2(f32x2 v) {
    bf16x2 b = __builtin_convertvector(v, bf16x2);
    return __builtin_bit_cast(unsigned int, b);
}
__device__ __forceinline__ f32x2 rnd2(f32x2 v) { return unpack2(pack2(v)); }

__device__ __forceinline__ u32x4 ld_nt(const char* base, unsigned off) {
    return __builtin_nontemporal_load((const u32x4*)(base + off));
}

#define KEEP_SCALE ((float)(1.0 / 0.9))

template <unsigned DBYTES>
__global__ __launch_bounds__(BLOCK) void fbda_vec_kernel(const char* __restrict__ x,
                                                         const char* __restrict__ mask,
                                                         const char* __restrict__ res,
                                                         const char* __restrict__ bias,
                                                         char* __restrict__ out) {
    static_assert((BLOCK * 16) % DBYTES == 0 && (DBYTES & (DBYTES - 1)) == 0 && DBYTES >= 16, "");
    constexpr unsigned USTEP = BLOCK * 16;
    const unsigned off = blockIdx.x * (USTEP * UNROLL) + threadIdx.x * 16;
    const unsigned boff = (threadIdx.x * 16) & (DBYTES - 1);
    u32x4 vx[UNROLL], vm[UNROLL], vr[UNROLL];
    u32x4 vb;
#pragma unroll
    for (int u = 0; u < UNROLL; ++u) {
        vx[u] = ld_nt(x, off + u * USTEP);
        if (u == 0) vb = *(const u32x4*)(bias + boff);
        vm[u] = ld_nt(mask, off + u * USTEP);
        vr[u] = ld_nt(res, off + u * USTEP);
    }
    __builtin_amdgcn_sched_barrier(0);
    f32x2 b[4];
#pragma unroll
    for (int u = 0; u < UNROLL; ++u) {
        f32x2 t[4];
        if (u == 0) {
#pragma unroll
            for (int j = 0; j < 4; ++j) b[j] = unpack2(vb[j]);
        }
#pragma unroll
        for (int j = 0; j < 4; ++j) t[j] = rnd2(unpack2(vx[u][j]) + b[j]);
        __builtin_amdgcn_sched_barrier(0);
#pragma unroll
        for (int j = 0; j < 4; ++j) t[j] = rnd2(rnd2(t[j] * unpack2(vm[u][j])) * KEEP_SCALE);
        __builtin_amdgcn_sched_barrier(0);
        u32x4 o;
#pragma unroll
        for (int j = 0; j < 4; ++j) o[j] = pack2(unpack2(vr[u][j]) + t[j]);
        *(u32x4*)(out + off + u * USTEP) = o;
        __builtin_amdgcn_sched_barrier(0);
    }
}

__global__ __launch_bounds__(256) void fbda_generic_kernel(const unsigned short* __restrict__ x,
                                                           const unsigned short* __restrict__ mask,
                                                           const unsigned short* __restrict__ res,
                                                           const unsigned short* __restrict__ bias,
                                                           unsigned short* __restrict__ out,
                                                           long start, long n, long D) {
    const long stride = (long)gridDim.x * 256;
    for (long i = start + (long)blockIdx.x * 256 + threadIdx.x; i < n; i += stride) {
        f32x2 t = rnd2(unpack2(x[i]) + unpack2(bias[i % D]));
        t = rnd2(rnd2(t * unpack2(mask[i])) * KEEP_SCALE);
        out[i] = (unsigned short)(pack2(unpack2(res[i]) + t) & 0xffffu);
    }
}

extern "C" void kernel_launch(void* const* d_in, const int* in_sizes, int n_in, void* d_out,
                              int out_size, void* d_ws, size_t ws_size, hipStream_t stream) {
    const long n = in_sizes[0];
    const long D = in_sizes[1];
    const char* x = (const char*)d_in[0];
    const char* bias = (const char*)d_in[1];
    const char* mask = (const char*)d_in[2];
    const char* res = (const char*)d_in[3];
    char* out = (char*)d_out;
    if (n <= 0 || D <= 0) return;

    const long per_block = (long)BLOCK * UNROLL * 8;
    const bool aligned = (((uintptr_t)x | (uintptr_t)bias | (uintptr_t)mask | (uintptr_t)res |
                           (uintptr_t)out) & 15) == 0;
    long done = 0;
    if (aligned && D == 1024 && n < (1L << 31)) {
        const long nblk = n / per_block;
        if (nblk > 0) {
            fbda_vec_kernel<2048><<<dim3((unsigned)nblk), dim3(BLOCK), 0, stream>>>(x, mask, res,
                                                                                     bias, out);
            done = nblk * per_block;
        }
    }
    if (done < n) {
        long blocks = (n - done + 255) / 256;
        if (blocks > 8192) blocks = 8192;
        fbda_generic_kernel<<<dim3((unsigned)blocks), dim3(256), 0, stream>>>(
            (const unsigned short*)x, (const unsigned short*)mask, (const unsigned short*)res,
            (const unsigned short*)bias, (unsigned short*)out, done, n, D);
    }
}
```

```cpp
#include <hip/hip_runtime.h>
#include <hip/hip_bf16.h>
#include <stdint.h>

typedef unsigned int u32x4 __attribute__((ext_vector_type(4)));
typedef float f32x2 __attribute__((ext_vector_type(2)));
typedef __bf16 bf16x2 __attribute__((ext_vector_type(2)));

#define BLOCK 512
#define KEEP_SCALE ((float)(1.0 / 0.9))

__device__ __forceinline__ f32x2 unpack2(unsigned int v) {
    f32x2 r;
    r.x = __uint_as_float(v << 16);
    r.y = __uint_as_float(v & 0xffff0000u);
    return r;
}
__device__ __forceinline__ unsigned int pack2(f32x2 v) {
    bf16x2 b = __builtin_convertvector(v, bf16x2);
    return __builtin_bit_cast(unsigned int, b);
}
__device__ __forceinline__ f32x2 rnd2(f32x2 v) { return unpack2(pack2(v)); }

__device__ __forceinline__ u32x4 ld_nt(const char* base, unsigned off) {
    return __builtin_nontemporal_load((const u32x4*)(base + off));
}

template <int U>
__device__ __forceinline__ void fbda_tiles(const char* __restrict__ x, const char* __restrict__ mask,
                                           const char* __restrict__ res,
                                           const char* __restrict__ bias, char* __restrict__ out,
                                           unsigned off, unsigned boff) {
    constexpr unsigned USTEP = 4 * BLOCK * 16;
    u32x4 vx[U], vm[U], vr[U], vb;
#pragma unroll
    for (int u = 0; u < U; ++u) {
        vx[u] = ld_nt(x, off + u * USTEP);
        if (u == 0) vb = *(const u32x4*)(bias + boff);
        vm[u] = ld_nt(mask, off + u * USTEP);
        vr[u] = ld_nt(res, off + u * USTEP);
    }
    __builtin_amdgcn_sched_barrier(0);
    f32x2 b[4];
#pragma unroll
    for (int u = 0; u < U; ++u) {
        f32x2 t[4];
        if (u == 0) {
#pragma unroll
            for (int j = 0; j < 4; ++j) b[j] = unpack2(vb[j]);
        }
#pragma unroll
        for (int j = 0; j < 4; ++j) t[j] = rnd2(unpack2(vx[u][j]) + b[j]);
        __builtin_amdgcn_sched_barrier(0);
#pragma unroll
        for (int j = 0; j < 4; ++j) t[j] = rnd2(rnd2(t[j] * unpack2(vm[u][j])) * KEEP_SCALE);
        __builtin_amdgcn_sched_barrier(0);
        u32x4 o;
#pragma unroll
        for (int j = 0; j < 4; ++j) o[j] = pack2(unpack2(vr[u][j]) + t[j]);
        *(u32x4*)(out + off + u * USTEP) = o;
        __builtin_amdgcn_sched_barrier(0);
    }
}

template <unsigned DBYTES, unsigned SHIFT_BLOCKS>
__global__ __launch_bounds__(BLOCK) void fbda_vec_kernel(const char* __restrict__ x,
                                                         const char* __restrict__ mask,
                                                         const char* __restrict__ res,
                                                         const char* __restrict__ bias,
                                                         char* __restrict__ out) {
    static_assert((BLOCK * 16) % DBYTES == 0 && (DBYTES & (DBYTES - 1)) == 0 && DBYTES >= 16, "");
    static_assert(SHIFT_BLOCKS % 8 == 0, "");
    const unsigned off = blockIdx.x * (BLOCK * 16) + threadIdx.x * 16;
    const unsigned boff = (threadIdx.x * 16) & (DBYTES - 1);
    if (SHIFT_BLOCKS > 0) {
        const unsigned wave = __builtin_amdgcn_readfirstlane(threadIdx.x >> 6);
        if (blockIdx.x < SHIFT_BLOCKS && wave == BLOCK / 64 - 1) {
            if (blockIdx.x & 4) return;
            fbda_tiles<2>(x, mask, res, bias, out, off, boff);
            return;
        }
    }
    fbda_tiles<1>(x, mask, res, bias, out, off, boff);
}

__global__ __launch_bounds__(256) void fbda_generic_kernel(const unsigned short* __restrict__ x,
                                                           const unsigned short* __restrict__ mask,
                                                           const unsigned short* __restrict__ res,
                                                           const unsigned short* __restrict__ bias,
                                                           unsigned short* __restrict__ out,
                                                           long start, long n, long D) {
    const long stride = (long)gridDim.x * 256;
    for (long i = start + (long)blockIdx.x * 256 + threadIdx.x; i < n; i += stride) {
        f32x2 t = rnd2(unpack2(x[i]) + unpack2(bias[i % D]));
        t = rnd2(rnd2(t * unpack2(mask[i])) * KEEP_SCALE);
        out[i] = (unsigned short)(pack2(unpack2(res[i]) + t) & 0xffffu);
    }
}

extern "C" void kernel_launch(void* const* d_in, const int* in_sizes, int n_in, void* d_out,
                              int out_size, void* d_ws, size_t ws_size, hipStream_t stream) {
    const long n = in_sizes[0];
    const long D = in_sizes[1];
    const char* x = (const char*)d_in[0];
    const char* bias = (const char*)d_in[1];
    const char* mask = (const char*)d_in[2];
    const char* res = (const char*)d_in[3];
    char* out = (char*)d_out;
    if (n <= 0 || D <= 0) return;

    const long per_block = (long)BLOCK * 8;
    const bool aligned = (((uintptr_t)x | (uintptr_t)bias | (uintptr_t)mask | (uintptr_t)res |
                           (uintptr_t)out) & 15) == 0;
    long done = 0;
    if (aligned && D == 1024) {
        const long nblk = n / per_block;
        if (nblk >= 1024) {
            fbda_vec_kernel<2048, 256><<<dim3((unsigned)nblk), dim3(BLOCK), 0, stream>>>(
                x, mask, res, bias, out);
        } else if (nblk > 0) {
            fbda_vec_kernel<2048, 0><<<dim3((unsigned)nblk), dim3(BLOCK), 0, stream>>>(
                x, mask, res, bias, out);
        }
        done = nblk * per_block;
    }
    if (done < n) {
        long blocks = (n - done + 255) / 256;
        if (blocks > 8192) blocks = 8192;
        fbda_generic_kernel<<<dim3((unsigned)blocks), dim3(256), 0, stream>>>(
            (const unsigned short*)x, (const unsigned short*)mask, (const unsigned short*)res,
            (const unsigned short*)bias, (unsigned short*)out, done, n, D);
    }
}
```

```cpp
#include <hip/hip_runtime.h>
#include <hip/hip_bf16.h>
#include <stdint.h>

typedef unsigned int u32x4 __attribute__((ext_vector_type(4)));
typedef float f32x2 __attribute__((ext_vector_type(2)));
typedef __bf16 bf16x2 __attribute__((ext_vector_type(2)));

#define BLOCK 512
#define KEEP_SCALE ((float)(1.0 / 0.9))

__device__ __forceinline__ f32x2 unpack2(unsigned int v) {
    f32x2 r;
    r.x = __uint_as_float(v << 16);
    r.y = __uint_as_float(v & 0xffff0000u);
    return r;
}
__device__ __forceinline__ unsigned int pack2(f32x2 v) {
    bf16x2 b = __builtin_convertvector(v, bf16x2);
    return __builtin_bit_cast(unsigned int, b);
}
__device__ __forceinline__ f32x2 rnd2(f32x2 v) { return unpack2(pack2(v)); }

__device__ __forceinline__ u32x4 ld_nt(const char* base, unsigned off) {
    return __builtin_nontemporal_load((const u32x4*)(base + off));
}

__device__ __forceinline__ u32x4 fbda_tile(const u32x4 vx, const f32x2 (&b)[4], const u32x4 vm,
                                           const u32x4 vr) {
    f32x2 t[4];
#pragma unroll
    for (int j = 0; j < 4; ++j) t[j] = rnd2(unpack2(vx[j]) + b[j]);
    __builtin_amdgcn_sched_barrier(0);
#pragma unroll
    for (int j = 0; j < 4; ++j) t[j] = rnd2(rnd2(t[j] * unpack2(vm[j])) * KEEP_SCALE);
    __builtin_amdgcn_sched_barrier(0);
    u32x4 o;
#pragma unroll
    for (int j = 0; j < 4; ++j) o[j] = pack2(unpack2(vr[j]) + t[j]);
    return o;
}

template <unsigned DBYTES, unsigned SHIFT_BLOCKS>
__global__ __launch_bounds__(BLOCK) void fbda_vec_kernel(const char* __restrict__ x,
                                                         const char* __restrict__ mask,
                                                         const char* __restrict__ res,
                                                         const char* __restrict__ bias,
                                                         char* __restrict__ out) {
    static_assert((BLOCK * 16) % DBYTES == 0 && (DBYTES & (DBYTES - 1)) == 0 && DBYTES >= 16, "");
    static_assert(SHIFT_BLOCKS % 8 == 0, "");
    constexpr unsigned EXTRA = 4 * BLOCK * 16;
    const unsigned off = blockIdx.x * (BLOCK * 16) + threadIdx.x * 16;
    const unsigned boff = (threadIdx.x * 16) & (DBYTES - 1);
    const unsigned t0 = __builtin_amdgcn_readfirstlane(threadIdx.x);
    const unsigned first = BLOCK - 64 * ((0x1123u >> ((blockIdx.x & 3) * 4)) & 15u);
    const bool shift = (SHIFT_BLOCKS > 0) & (blockIdx.x < SHIFT_BLOCKS) & (t0 >= first);
    asm volatile("" ::"s"(x), "s"(mask), "s"(res), "s"(bias), "s"(out), "v"(off), "v"(boff));
    if (shift & ((blockIdx.x & 4) != 0)) return;
    const u32x4 vb = *(const u32x4*)(bias + boff);
    __builtin_amdgcn_sched_barrier(0);
    const u32x4 vx = ld_nt(x, off);
    __builtin_amdgcn_sched_barrier(0);
    const u32x4 vm = ld_nt(mask, off);
    __builtin_amdgcn_sched_barrier(0);
    const u32x4 vr = ld_nt(res, off);
    __builtin_amdgcn_sched_barrier(0);
    u32x4 ex, em, er;
    if (shift) {
        ex = ld_nt(x, off + EXTRA);
        em = ld_nt(mask, off + EXTRA);
        er = ld_nt(res, off + EXTRA);
    }
    __builtin_amdgcn_sched_barrier(0);
    f32x2 b[4];
#pragma unroll
    for (int j = 0; j < 4; ++j) b[j] = unpack2(vb[j]);
    *(u32x4*)(out + off) = fbda_tile(vx, b, vm, vr);
    if (shift) *(u32x4*)(out + off + EXTRA) = fbda_tile(ex, b, em, er);
}

__global__ __launch_bounds__(256) void fbda_generic_kernel(const unsigned short* __restrict__ x,
                                                           const unsigned short* __restrict__ mask,
                                                           const unsigned short* __restrict__ res,
                                                           const unsigned short* __restrict__ bias,
                                                           unsigned short* __restrict__ out,
                                                           long start, long n, long D) {
    const long stride = (long)gridDim.x * 256;
    for (long i = start + (long)blockIdx.x * 256 + threadIdx.x; i < n; i += stride) {
        f32x2 t = rnd2(unpack2(x[i]) + unpack2(bias[i % D]));
        t = rnd2(rnd2(t * unpack2(mask[i])) * KEEP_SCALE);
        out[i] = (unsigned short)(pack2(unpack2(res[i]) + t) & 0xffffu);
    }
}

extern "C" void kernel_launch(void* const* d_in, const int* in_sizes, int n_in, void* d_out,
                              int out_size, void* d_ws, size_t ws_size, hipStream_t stream) {
    const long n = in_sizes[0];
    const long D = in_sizes[1];
    const char* x = (const char*)d_in[0];
    const char* bias = (const char*)d_in[1];
    const char* mask = (const char*)d_in[2];
    const char* res = (const char*)d_in[3];
    char* out = (char*)d_out;
    if (n <= 0 || D <= 0) return;

    const long per_block = (long)BLOCK * 8;
    const bool aligned = (((uintptr_t)x | (uintptr_t)bias | (uintptr_t)mask | (uintptr_t)res |
                           (uintptr_t)out) & 15) == 0;
    long done = 0;
    if (aligned && D == 1024) {
        const long nblk = n / per_block;
        if (nblk >= 1024) {
            fbda_vec_kernel<2048, 256><<<dim3((unsigned)nblk), dim3(BLOCK), 0, stream>>>(
                x, mask, res, bias, out);
        } else if (nblk > 0) {
            fbda_vec_kernel<2048, 0><<<dim3((unsigned)nblk), dim3(BLOCK), 0, stream>>>(
                x, mask, res, bias, out);
        }
        done = nblk * per_block;
    }
    if (done < n) {
        long blocks = (n - done + 255) / 256;
        if (blocks > 8192) blocks = 8192;
        fbda_generic_kernel<<<dim3((unsigned)blocks), dim3(256), 0, stream>>>(
            (const unsigned short*)x, (const unsigned short*)mask, (const unsigned short*)res,
            (const unsigned short*)bias, (unsigned short*)out, done, n, D);
    }
}
```

```cpp
#include <hip/hip_runtime.h>
#include <hip/hip_bf16.h>
#include <stdint.h>

typedef unsigned int u32x4 __attribute__((ext_vector_type(4)));
typedef float f32x2 __attribute__((ext_vector_type(2)));
typedef __bf16 bf16x2 __attribute__((ext_vector_type(2)));

#define BLOCK 512
#define KEEP_SCALE ((float)(1.0 / 0.9))

__device__ __forceinline__ f32x2 unpack2(unsigned int v) {
    f32x2 r;
    r.x = __uint_as_float(v << 16);
    r.y = __uint_as_float(v & 0xffff0000u);
    return r;
}
__device__ __forceinline__ unsigned int pack2(f32x2 v) {
    bf16x2 b = __builtin_convertvector(v, bf16x2);
    return __builtin_bit_cast(unsigned int, b);
}
__device__ __forceinline__ f32x2 rnd2(f32x2 v) { return unpack2(pack2(v)); }

__device__ __forceinline__ u32x4 ld_nt(const char* base, unsigned off) {
    return __builtin_nontemporal_load((const u32x4*)(base + off));
}

__device__ __forceinline__ u32x4 fbda_tile(const u32x4 vx, const f32x2 (&b)[4], const u32x4 vm,
                                           const u32x4 vr) {
    f32x2 t[4];
#pragma unroll
    for (int j = 0; j < 4; ++j) t[j] = rnd2(unpack2(vx[j]) + b[j]);
    __builtin_amdgcn_sched_barrier(0);
#pragma unroll
    for (int j = 0; j < 4; ++j) t[j] = rnd2(rnd2(t[j] * unpack2(vm[j])) * KEEP_SCALE);
    __builtin_amdgcn_sched_barrier(0);
    u32x4 o;
#pragma unroll
    for (int j = 0; j < 4; ++j) o[j] = pack2(unpack2(vr[j]) + t[j]);
    return o;
}

template <unsigned DBYTES, unsigned SHIFT_BLOCKS>
__global__ __launch_bounds__(BLOCK) void fbda_vec_kernel(const char* __restrict__ x,
                                                         const char* __restrict__ mask,
                                                         const char* __restrict__ res,
                                                         const char* __restrict__ bias,
                                                         char* __restrict__ out) {
    static_assert((BLOCK * 16) % DBYTES == 0 && (DBYTES & (DBYTES - 1)) == 0 && DBYTES >= 16, "");
    static_assert(SHIFT_BLOCKS % 8 == 0, "");
    const unsigned long long tstart = __builtin_amdgcn_s_memrealtime();
    __builtin_amdgcn_sched_barrier(0);
    constexpr unsigned EXTRA = 4 * BLOCK * 16;
    const unsigned off = blockIdx.x * (BLOCK * 16) + threadIdx.x * 16;
    const unsigned boff = (threadIdx.x * 16) & (DBYTES - 1);
    const unsigned t0 = __builtin_amdgcn_readfirstlane(threadIdx.x);
    const unsigned first = (blockIdx.x & 3) ? BLOCK - 64 : BLOCK - 128;
    const bool shift = (SHIFT_BLOCKS > 0) & (blockIdx.x < SHIFT_BLOCKS) & (t0 >= first);
    asm volatile("" ::"s"(x), "s"(mask), "s"(res), "s"(bias), "s"(out), "v"(off), "v"(boff));
    if (shift & ((blockIdx.x & 4) != 0)) return;
    const u32x4 vb = *(const u32x4*)(bias + boff);
    __builtin_amdgcn_sched_barrier(0);
    const u32x4 vx = ld_nt(x, off);
    __builtin_amdgcn_sched_barrier(0);
    const u32x4 vm = ld_nt(mask, off);
    __builtin_amdgcn_sched_barrier(0);
    const u32x4 vr = ld_nt(res, off);
    __builtin_amdgcn_sched_barrier(0);
    u32x4 ex, em, er;
    if (shift) {
        ex = ld_nt(x, off + EXTRA);
        em = ld_nt(mask, off + EXTRA);
        er = ld_nt(res, off + EXTRA);
    }
    __builtin_amdgcn_sched_barrier(0);
    f32x2 b[4];
#pragma unroll
    for (int j = 0; j < 4; ++j) b[j] = unpack2(vb[j]);
    *(u32x4*)(out + off) = fbda_tile(vx, b, vm, vr);
    if (shift) *(u32x4*)(out + off + EXTRA) = fbda_tile(ex, b, em, er);
    if (blockIdx.x < 256 && t0 == 0) {
        for (int i = 0; i < 4000; ++i) {
            if (__builtin_amdgcn_s_memrealtime() - tstart >= 800) break;
            __builtin_amdgcn_s_sleep(2);
        }
    }
}

__global__ __launch_bounds__(256) void fbda_generic_kernel(const unsigned short* __restrict__ x,
                                                           const unsigned short* __restrict__ mask,
                                                           const unsigned short* __restrict__ res,
                                                           const unsigned short* __restrict__ bias,
                                                           unsigned short* __restrict__ out,
                                                           long start, long n, long D) {
    const long stride = (long)gridDim.x * 256;
    for (long i = start + (long)blockIdx.x * 256 + threadIdx.x; i < n; i += stride) {
        f32x2 t = rnd2(unpack2(x[i]) + unpack2(bias[i % D]));
        t = rnd2(rnd2(t * unpack2(mask[i])) * KEEP_SCALE);
        out[i] = (unsigned short)(pack2(unpack2(res[i]) + t) & 0xffffu);
    }
}

extern "C" void kernel_launch(void* const* d_in, const int* in_sizes, int n_in, void* d_out,
                              int out_size, void* d_ws, size_t ws_size, hipStream_t stream) {
    const long n = in_sizes[0];
    const long D = in_sizes[1];
    const char* x = (const char*)d_in[0];
    const char* bias = (const char*)d_in[1];
    const char* mask = (const char*)d_in[2];
    const char* res = (const char*)d_in[3];
    char* out = (char*)d_out;
    if (n <= 0 || D <= 0) return;

    const long per_block = (long)BLOCK * 8;
    const bool aligned = (((uintptr_t)x | (uintptr_t)bias | (uintptr_t)mask | (uintptr_t)res |
                           (uintptr_t)out) & 15) == 0;
    long done = 0;
    if (aligned && D == 1024) {
        const long nblk = n / per_block;
        if (nblk >= 1024) {
            fbda_vec_kernel<2048, 256><<<dim3((unsigned)nblk), dim3(BLOCK), 0, stream>>>(
                x, mask, res, bias, out);
        } else if (nblk > 0) {
            fbda_vec_kernel<2048, 0><<<dim3((unsigned)nblk), dim3(BLOCK), 0, stream>>>(
                x, mask, res, bias, out);
        }
        done = nblk * per_block;
    }
    if (done < n) {
        long blocks = (n - done + 255) / 256;
        if (blocks > 8192) blocks = 8192;
        fbda_generic_kernel<<<dim3((unsigned)blocks), dim3(256), 0, stream>>>(
            (const unsigned short*)x, (const unsigned short*)mask, (const unsigned short*)res,
            (const unsigned short*)bias, (unsigned short*)out, done, n, D);
    }
}
```

```cpp
#include <hip/hip_runtime.h>
#include <hip/hip_bf16.h>
#include <stdint.h>

typedef unsigned int u32x4 __attribute__((ext_vector_type(4)));
typedef float f32x2 __attribute__((ext_vector_type(2)));
typedef __bf16 bf16x2 __attribute__((ext_vector_type(2)));

#define BLOCK 512
#define KEEP_SCALE ((float)(1.0 / 0.9))

__device__ __forceinline__ f32x2 unpack2(unsigned int v) {
    f32x2 r;
    r.x = __uint_as_float(v << 16);
    r.y = __uint_as_float(v & 0xffff0000u);
    return r;
}
__device__ __forceinline__ unsigned int pack2(f32x2 v) {
    bf16x2 b = __builtin_convertvector(v, bf16x2);
    return __builtin_bit_cast(unsigned int, b);
}
__device__ __forceinline__ f32x2 rnd2(f32x2 v) { return unpack2(pack2(v)); }

__device__ __forceinline__ u32x4 ld_nt(const char* base, unsigned off) {
    return __builtin_nontemporal_load((const u32x4*)(base + off));
}

__device__ __forceinline__ u32x4 fbda_tile(const u32x4 vx, const f32x2 (&b)[4], const u32x4 vm,
                                           const u32x4 vr) {
    f32x2 t[4];
#pragma unroll
    for (int j = 0; j < 4; ++j) t[j] = rnd2(unpack2(vx[j]) + b[j]);
    __builtin_amdgcn_sched_barrier(0);
#pragma unroll
    for (int j = 0; j < 4; ++j) t[j] = rnd2(rnd2(t[j] * unpack2(vm[j])) * KEEP_SCALE);
    __builtin_amdgcn_sched_barrier(0);
    u32x4 o;
#pragma unroll
    for (int j = 0; j < 4; ++j) o[j] = pack2(unpack2(vr[j]) + t[j]);
    return o;
}

template <unsigned DBYTES, unsigned SHIFT_BLOCKS>
__global__ __launch_bounds__(BLOCK) void fbda_vec_kernel(const char* __restrict__ x,
                                                         const char* __restrict__ mask,
                                                         const char* __restrict__ res,
                                                         const char* __restrict__ bias,
                                                         char* __restrict__ out) {
    static_assert((BLOCK * 16) % DBYTES == 0 && (DBYTES & (DBYTES - 1)) == 0 && DBYTES >= 16, "");
    static_assert(SHIFT_BLOCKS % 8 == 0, "");
    constexpr unsigned EXTRA = 4 * BLOCK * 16;
    const unsigned off = blockIdx.x * (BLOCK * 16) + threadIdx.x * 16;
    const unsigned boff = (threadIdx.x * 16) & (DBYTES - 1);
    const unsigned t0 = __builtin_amdgcn_readfirstlane(threadIdx.x);
    const unsigned first = (blockIdx.x & 3) ? BLOCK - 64 : BLOCK - 128;
    const bool shift = (SHIFT_BLOCKS > 0) & (blockIdx.x < SHIFT_BLOCKS) & (t0 >= first);
    asm volatile("" ::"s"(x), "s"(mask), "s"(res), "s"(bias), "s"(out), "v"(off), "v"(boff));
    if (shift & ((blockIdx.x & 4) != 0)) return;
    const u32x4 vb = *(const u32x4*)(bias + boff);
    __builtin_amdgcn_sched_barrier(0);
    const u32x4 vx = ld_nt(x, off);
    __builtin_amdgcn_sched_barrier(0);
    const u32x4 vm = ld_nt(mask, off);
    __builtin_amdgcn_sched_barrier(0);
    const u32x4 vr = ld_nt(res, off);
    __builtin_amdgcn_sched_barrier(0);
    u32x4 ex, em, er;
    if (shift) {
        ex = ld_nt(x, off + EXTRA);
        em = ld_nt(mask, off + EXTRA);
        er = ld_nt(res, off + EXTRA);
    }
    __builtin_amdgcn_sched_barrier(0);
    f32x2 b[4];
#pragma unroll
    for (int j = 0; j < 4; ++j) b[j] = unpack2(vb[j]);
    *(u32x4*)(out + off) = fbda_tile(vx, b, vm, vr);
    if (shift) *(u32x4*)(out + off + EXTRA) = fbda_tile(ex, b, em, er);
}

__global__ __launch_bounds__(256) void fbda_generic_kernel(const unsigned short* __restrict__ x,
                                                           const unsigned short* __restrict__ mask,
                                                           const unsigned short* __restrict__ res,
                                                           const unsigned short* __restrict__ bias,
                                                           unsigned short* __restrict__ out,
                                                           long start, long n, long D) {
    const long stride = (long)gridDim.x * 256;
    for (long i = start + (long)blockIdx.x * 256 + threadIdx.x; i < n; i += stride) {
        f32x2 t = rnd2(unpack2(x[i]) + unpack2(bias[i % D]));
        t = rnd2(rnd2(t * unpack2(mask[i])) * KEEP_SCALE);
        out[i] = (unsigned short)(pack2(unpack2(res[i]) + t) & 0xffffu);
    }
}

extern "C" void kernel_launch(void* const* d_in, const int* in_sizes, int n_in, void* d_out,
                              int out_size, void* d_ws, size_t ws_size, hipStream_t stream) {
    const long n = in_sizes[0];
    const long D = in_sizes[1];
    const char* x = (const char*)d_in[0];
    const char* bias = (const char*)d_in[1];
    const char* mask = (const char*)d_in[2];
    const char* res = (const char*)d_in[3];
    char* out = (char*)d_out;
    if (n <= 0 || D <= 0) return;

    const long per_block = (long)BLOCK * 8;
    const bool aligned = (((uintptr_t)x | (uintptr_t)bias | (uintptr_t)mask | (uintptr_t)res |
                           (uintptr_t)out) & 15) == 0;
    long done = 0;
    if (aligned && D == 1024) {
        const long nblk = n / per_block;
        if (nblk >= 1024) {
            fbda_vec_kernel<2048, 256><<<dim3((unsigned)nblk), dim3(BLOCK), 0, stream>>>(
                x, mask, res, bias, out);
        } else if (nblk > 0) {
            fbda_vec_kernel<2048, 0><<<dim3((unsigned)nblk), dim3(BLOCK), 0, stream>>>(
                x, mask, res, bias, out);
        }
        done = nblk * per_block;
    }
    if (done < n) {
        long blocks = (n - done + 255) / 256;
        if (blocks > 8192) blocks = 8192;
        fbda_generic_kernel<<<dim3((unsigned)blocks), dim3(256), 0, stream>>>(
            (const unsigned short*)x, (const unsigned short*)mask, (const unsigned short*)res,
            (const unsigned short*)bias, (unsigned short*)out, done, n, D);
    }
}
```
